```python
import torch

def _softmax(x, axis=-1):
    return torch.softmax(x, dim=axis)
def _gelu(x):
    return torch.nn.functional.gelu(x, approximate="tanh")
def _silu(x):
    return torch.nn.functional.silu(x)
def _sigmoid(x):
    return torch.sigmoid(x)
def _layernorm(x, axis=-1, eps=1e-5):
    m = x.mean(dim=axis, keepdim=True)
    v = x.var(dim=axis, keepdim=True, unbiased=False)
    return (x - m) / torch.sqrt(v + eps)
def _scatter_add(tgt, idx, src, axis=0):
    return tgt.clone().index_add_(axis, idx, src)
def _segment_sum(src, seg, n):
    out = torch.zeros((n,) + src.shape[1:], dtype=src.dtype)
    return out.index_add_(0, seg, src)
def _pad(x, pw):
    flat = tuple(v for pair in reversed(pw) for v in pair)
    return torch.nn.functional.pad(x, flat)

kernel_name = 'sequential_preLN__gqa_block_cached__glu_gated_bilinear__decode'

def setup_inputs():

    _w = (lambda a: (lambda t: t.to(torch.bfloat16) if t.is_floating_point() else t)(torch.as_tensor(a)))
    def _A_setup():
        B, Sq, D = 1, 1, 512
        Hq, Hkv, Sk, Dh = 32, 8, 65536, 128
        assert Hq % Hkv == 0, (Hq, Hkv)
        return {
            "x": _w((lambda *s: torch.randn(*s, dtype=torch.bfloat16) * 0.05)(B, Sq, D)),
            "k_cache": _w((lambda *s: torch.randn(*s, dtype=torch.bfloat16) * 3.2)(B, Hkv, Sk, Dh)),
            "v_cache": _w((lambda *s: torch.randn(*s, dtype=torch.bfloat16) * 1.6)(B, Hkv, Sk, Dh)),
            "wq": _w((lambda *s: torch.randn(*s, dtype=torch.bfloat16) * 3.2)(D, Hq * Dh) * (D ** -0.5)),
            "wo": _w((lambda *s: torch.randn(*s, dtype=torch.bfloat16) * 1.6)(Hq * Dh, D) * ((Hq * Dh) ** -0.5)),
        }
    _A_in = _A_setup()

    def _B_setup():
        B, S, D, F = 1, 1, 512, 1792
        return {
            "x": _w((lambda *s: torch.randn(*s, dtype=torch.bfloat16) * 0.05)(B, S, D)),
            "w_gate": _w((lambda *s: torch.randn(*s, dtype=torch.bfloat16) * 1.6)(D, F) * (D ** -0.5)),
            "w_up": _w((lambda *s: torch.randn(*s, dtype=torch.bfloat16) * 1.6)(D, F) * (D ** -0.5)),
            "w_down": _w((lambda *s: torch.randn(*s, dtype=torch.bfloat16) * 1.6)(F, D) * (F ** -0.5)),
        }
    _B_in = _B_setup()

    return {
        "A_x": _A_in["x"],
        "A_k_cache": _A_in["k_cache"],
        "A_v_cache": _A_in["v_cache"],
        "A_wq": _A_in["wq"],
        "A_wo": _A_in["wo"],
        "B_x": _B_in["x"],
        "B_w_gate": _B_in["w_gate"],
        "B_w_up": _B_in["w_up"],
        "B_w_down": _B_in["w_down"],
    }

def reference(A_x, A_k_cache, A_v_cache, A_wq, A_wo, B_x, B_w_gate, B_w_up, B_w_down):
    def _A_ref(x, k_cache, v_cache, wq, wo):
        B, Sq, D = x.shape
        Hq, Dh = 32, 128
        Hkv = k_cache.shape[1]
        rep = Hq // Hkv
        q = (lambda x, *d: x.permute(*d))((x @ wq).reshape(B, Sq, Hq, Dh), 0, 2, 1, 3)
        k = (lambda x, n, axis: torch.repeat_interleave(x, n, dim=axis))(k_cache, rep, axis=1)
        v = (lambda x, n, axis: torch.repeat_interleave(x, n, dim=axis))(v_cache, rep, axis=1)
        scores = torch.einsum("bhqd,bhkd->bhqk", q, k) * (Dh ** -0.5)
        p = _softmax(scores, axis=-1)
        o = torch.einsum("bhqk,bhkd->bhqd", p, v)
        return (lambda x, *d: x.permute(*d))(o, 0, 2, 1, 3).reshape(B, Sq, Hq * Dh) @ wo

    def _B_ref(x, w_gate, w_up, w_down):
        g = torch.einsum("bsd,df->bsf", x, w_gate)
        u = torch.einsum("bsd,df->bsf", x, w_up)
        h = g * u
        return torch.einsum("bsf,fd->bsd", h, w_down)

    x = A_x
    h = x + _A_ref(_layernorm(x, axis=-1), A_k_cache, A_v_cache, A_wq, A_wo)
    h2 = h + _B_ref(_layernorm(h, axis=-1), B_w_gate, B_w_up, B_w_down)
    return h2
```

```cpp
#include <hip/hip_runtime.h>
#include <hip/hip_bf16.h>
#include <stdint.h>

#pragma clang diagnostic ignored "-Wint-to-pointer-cast"
namespace {

typedef unsigned u32;
typedef unsigned long long u64;
typedef float f32x4 __attribute__((ext_vector_type(4)));
typedef u32 u32x4 __attribute__((ext_vector_type(4)));
typedef u32 u32x2 __attribute__((ext_vector_type(2)));
typedef short s16x4 __attribute__((ext_vector_type(4)));
typedef __bf16 bf16x8 __attribute__((ext_vector_type(8)));
typedef __bf16 bf16x2 __attribute__((ext_vector_type(2)));
#define LAS __attribute__((address_space(3)))
#define GAS __attribute__((address_space(1)))
typedef __amdgpu_buffer_rsrc_t rsrc_t;

constexpr int DM = 512, SK = 65536, FF = 1792;
constexpr int NWG = 256, NTHR = 512;
constexpr int NTILE = 32;
constexpr u32 SENT = 0xFFFFFFFFu;
constexpr float QSCALE = 0.08838834764831845f * 1.4426950408889634f;
constexpr u32 SPIN_CAP = 1u << 17;

#define SLP 0
#define FLAGT 22
#define DEEP 0
#define COMPUTE_QK const f32x4 S = qk(s, so)
#define COMPUTE_SM readV(s, so, vf); __builtin_amdgcn_sched_barrier(0); const s16x4 pb = softmax_step(s, S)
#define COMPUTE_PV pv(s, vf, pb)
#define QSLP 12
#define SDELAY 1
#define NSLOT 4
constexpr int NS = NSLOT;
constexpr int L_RING = 0;
constexpr int RINGB = NS * 8192;
constexpr int L_STG = 4 * RINGB;
constexpr int L_X = L_STG + 16384;
constexpr int L_XN = L_X + 4096;
constexpr int L_Q = L_XN + 1024;
constexpr int L_O = L_Q + 1024;
constexpr int L_GU = L_O + 1024;
constexpr int L_P = L_GU + 3584;
constexpr int L_WGT = L_P + 1024;
constexpr int L_ST = L_WGT + 512;
constexpr int L_H = L_ST + 64;
constexpr int L_CNT = L_H + 64;
constexpr int L_PROG = L_CNT + 4;
constexpr int L_END = L_CNT + 16;
static_assert(L_END <= 160 * 1024 && L_P % 16 == 0 && NS >= 3, "lds map");

constexpr u32 W_EPOCH = 0;
constexpr u32 W_FLAG = 2048;
constexpr u32 W_QG = 4096;
constexpr u32 W_YB = W_QG + 8 * 4 * 512 * 8;
constexpr u32 W_GU = W_YB + 8 * 512 * 4;
constexpr u32 W_ML = W_GU + 4096;
constexpr u32 W_ACC = W_ML + 8 * 4 * 32 * 16;
constexpr u32 W_END = W_ACC + 8 * 4 * 32 * 512;
#define STAMP(k) do { } while (0)
#define STAMP_DECL do { } while (0)
#define STAMP_FLUSH do { } while (0)

struct Params {
  const void* x; const void* k; const void* v; const void* wq; const void* wo;
  const void* wg; const void* wu; const void* wd; void* out; void* ws;
};

__device__ __forceinline__ u32 rfl(u32 x) { return __builtin_amdgcn_readfirstlane(x); }
__device__ __forceinline__ rsrc_t mk_rsrc(const void* p, u32 bytes) { return __builtin_amdgcn_make_buffer_rsrc((void*)p, (short)0, (int)bytes, 0x00020000); }
__device__ __forceinline__ float bf_lo(u32 w) { return __uint_as_float(w << 16); }
__device__ __forceinline__ float bf_hi(u32 w) { return __uint_as_float(w & 0xffff0000u); }
__device__ __forceinline__ u32 pk_bf16(float lo, float hi) { bf16x2 r = {(__bf16)lo, (__bf16)hi}; return __builtin_bit_cast(u32, r); }
__device__ __forceinline__ float rbf(float x) { return bf_lo(pk_bf16(x, 0.f)); }
__device__ __forceinline__ u32 nosent(u32 x) { return x == SENT ? 0x7FC07FC0u : x; }
__device__ __forceinline__ u32 fbits(float x) { return nosent(__float_as_uint(x)); }

#define WAITVM(n) asm volatile("s_waitcnt vmcnt(" #n ")" ::: "memory")
__device__ __forceinline__ void bar() { asm volatile("s_waitcnt lgkmcnt(0)\n\ts_barrier" ::: "memory"); }
__device__ __forceinline__ void waitlds() { asm volatile("s_waitcnt lgkmcnt(0)" ::: "memory"); }

__device__ __forceinline__ void dma1(u32 m0v, u32 voff, rsrc_t srd, u32 soff) {
  asm volatile("s_nop 4\n\ts_mov_b32 m0, %0\n\ts_nop 0\n\tbuffer_load_dwordx4 %1, %2, %3 offen nt lds" :: "s"(m0v), "v"(voff), "s"(srd), "s"(soff) : "memory");
}
__device__ __forceinline__ void dma4nt(u32 m0v, u32 v0, u32 v1, u32 v2, u32 v3, rsrc_t srd, u32 soff) {
  asm volatile("s_waitcnt lgkmcnt(0)\n\ts_mov_b32 m0, %0\n\ts_nop 0\n\t"
               "buffer_load_dwordx4 %1, %5, %6 offen nt lds\n\t"
               "buffer_load_dwordx4 %2, %5, %6 offen offset:1024 nt lds\n\t"
               "buffer_load_dwordx4 %3, %5, %6 offen offset:2048 nt lds\n\t"
               "buffer_load_dwordx4 %4, %5, %6 offen offset:3072 nt lds"
               :: "s"(m0v), "v"(v0), "v"(v1), "v"(v2), "v"(v3), "s"(srd), "s"(soff) : "memory");
}
constexpr int AUX_SC1 = 16;
__device__ __forceinline__ u32x4 ld128_sc1(rsrc_t r, u32 off) { return __builtin_amdgcn_raw_buffer_load_b128(r, off, 0, AUX_SC1); }
__device__ __forceinline__ void st128_sc1(u32x4 v, rsrc_t r, u32 off) { __builtin_amdgcn_raw_buffer_store_b128(v, r, off, 0, AUX_SC1); }
__device__ __forceinline__ void st32_sc1(u32 v, rsrc_t r, u32 off) { __builtin_amdgcn_raw_buffer_store_b32(v, r, off, 0, AUX_SC1); }
__device__ __forceinline__ bool valid4(u32x4 v) { return v.x != SENT && v.y != SENT && v.z != SENT && v.w != SENT; }

template <int CTRL> __device__ __forceinline__ float dpp(float x) {
  return __builtin_bit_cast(float, __builtin_amdgcn_mov_dpp(__builtin_bit_cast(int, x), CTRL, 0xf, 0xf, true));
}
constexpr int XOR1 = 0xB1, XOR2 = 0x4E, XOR7 = 0x141, XOR8 = 0x128, ROR4 = 0x124;
__device__ __forceinline__ float x16max(float x) {
  auto s = __builtin_amdgcn_permlane16_swap(__float_as_uint(x), __float_as_uint(x), false, false);
  x = fmaxf(__uint_as_float(s[0]), __uint_as_float(s[1]));
  auto t = __builtin_amdgcn_permlane32_swap(__float_as_uint(x), __float_as_uint(x), false, false);
  return fmaxf(__uint_as_float(t[0]), __uint_as_float(t[1]));
}
__device__ __forceinline__ float x16sum(float x) {
  auto s = __builtin_amdgcn_permlane16_swap(__float_as_uint(x), __float_as_uint(x), false, false);
  x = __uint_as_float(s[0]) + __uint_as_float(s[1]);
  auto t = __builtin_amdgcn_permlane32_swap(__float_as_uint(x), __float_as_uint(x), false, false);
  return __uint_as_float(t[0]) + __uint_as_float(t[1]);
}
__device__ __forceinline__ float x32sum(float x) {
  auto t = __builtin_amdgcn_permlane32_swap(__float_as_uint(x), __float_as_uint(x), false, false);
  return __uint_as_float(t[0]) + __uint_as_float(t[1]);
}
__device__ __forceinline__ float row16sum(float x) {
  x += dpp<XOR1>(x); x += dpp<XOR2>(x); x += dpp<XOR7>(x); x += dpp<XOR8>(x); return x;
}
__device__ __forceinline__ float row16max(float x) {
  x = fmaxf(x, dpp<XOR1>(x)); x = fmaxf(x, dpp<XOR2>(x)); x = fmaxf(x, dpp<XOR7>(x)); x = fmaxf(x, dpp<XOR8>(x)); return x;
}
__device__ __forceinline__ float wavesum(float x) { return x16sum(row16sum(x)); }

template <int ROWB> __device__ __forceinline__ bf16x8 wfrag(u32 a) {
  const s16x4 lo = __builtin_amdgcn_ds_read_tr16_b64_v4i16((LAS s16x4*)a);
  const s16x4 hi = __builtin_amdgcn_ds_read_tr16_b64_v4i16((LAS s16x4*)(a + 16 * ROWB));
  return __builtin_bit_cast(bf16x8, __builtin_shufflevector(lo, hi, 0, 1, 2, 3, 4, 5, 6, 7));
}
__device__ __forceinline__ bf16x8 vfrag(u32 vb) {
  const s16x4 lo = *(LAS s16x4*)vb, hi = *(LAS s16x4*)(vb + 32);
  return __builtin_bit_cast(bf16x8, __builtin_shufflevector(lo, hi, 0, 1, 2, 3, 4, 5, 6, 7));
}
__device__ __forceinline__ f32x4 mma(bf16x8 a, bf16x8 b, f32x4 c) { return __builtin_amdgcn_mfma_f32_16x16x32_bf16(a, b, c, 0, 0, 0); }
__device__ __forceinline__ u32 src128(int lane) { const int r = lane >> 3, s = lane & 7; return 16 * ((((s >> 1) ^ ((r >> 1) & 3)) << 1) | (s & 1)); }
__device__ __forceinline__ u32 src64(int lane) { const int r = lane >> 2, s = lane & 3; return 16 * ((((s >> 1) ^ ((r >> 2) & 1)) << 1) | (s & 1)); }

#define PL(i, reg, vo, so, off) "s_bitcmp1_b32 %[p], " #i "\n\ts_cbranch_scc0 .Lsk%=_" #i "\n\tbuffer_load_dwordx4 %[" reg "], %[" vo "], %[rs], " so " offen offset:" #off " sc1\n.Lsk%=_" #i ":\n\t"
__device__ __forceinline__ void poll17(u32x4 (&a)[16], u32x4& m, u32 pend, u32 vo, u32 vo2, rsrc_t rs, u32 s4k) {
  asm volatile(PL(16, "m", "v2", "0", 0)
               PL(0, "a0", "v", "0", 0) PL(1, "a1", "v", "0", 512) PL(2, "a2", "v", "0", 1024) PL(3, "a3", "v", "0", 1536)
               PL(4, "a4", "v", "0", 2048) PL(5, "a5", "v", "0", 2560) PL(6, "a6", "v", "0", 3072) PL(7, "a7", "v", "0", 3584)
               PL(8, "a8", "v", "%[s4]", 0) PL(9, "a9", "v", "%[s4]", 512) PL(10, "a10", "v", "%[s4]", 1024) PL(11, "a11", "v", "%[s4]", 1536)
               PL(12, "a12", "v", "%[s4]", 2048) PL(13, "a13", "v", "%[s4]", 2560) PL(14, "a14", "v", "%[s4]", 3072) PL(15, "a15", "v", "%[s4]", 3584)
               "s_waitcnt vmcnt(0)"
               : [a0] "+v"(a[0]), [a1] "+v"(a[1]), [a2] "+v"(a[2]), [a3] "+v"(a[3]), [a4] "+v"(a[4]), [a5] "+v"(a[5]), [a6] "+v"(a[6]), [a7] "+v"(a[7]),
                 [a8] "+v"(a[8]), [a9] "+v"(a[9]), [a10] "+v"(a[10]), [a11] "+v"(a[11]), [a12] "+v"(a[12]), [a13] "+v"(a[13]), [a14] "+v"(a[14]), [a15] "+v"(a[15]), [m] "+v"(m)
               : [p] "s"(pend), [v] "v"(vo), [v2] "v"(vo2), [rs] "s"(rs), [s4] "s"(s4k) : "scc", "memory");
}
__device__ __forceinline__ void poll4(u32x4 (&a)[4], u32 pend, u32 vo, rsrc_t rs, u32 s4k) {
  asm volatile(PL(0, "a0", "v", "0", 0) PL(1, "a1", "v", "0", 2048) PL(2, "a2", "v", "%[s4]", 0) PL(3, "a3", "v", "%[s4]", 2048)
               "s_waitcnt vmcnt(0)"
               : [a0] "+v"(a[0]), [a1] "+v"(a[1]), [a2] "+v"(a[2]), [a3] "+v"(a[3])
               : [p] "s"(pend), [v] "v"(vo), [rs] "s"(rs), [s4] "s"(s4k) : "scc", "memory");
}
__device__ __forceinline__ void poll4k(u32x4 (&a)[4], u32 pend, u32 vo, rsrc_t rs, u32 s1, u32 s2, u32 s3) {
  asm volatile(PL(0, "a0", "v", "0", 0) PL(1, "a1", "v", "%[s1]", 0) PL(2, "a2", "v", "%[s2]", 0) PL(3, "a3", "v", "%[s3]", 0)
               "s_waitcnt vmcnt(0)"
               : [a0] "+v"(a[0]), [a1] "+v"(a[1]), [a2] "+v"(a[2]), [a3] "+v"(a[3])
               : [p] "s"(pend), [v] "v"(vo), [rs] "s"(rs), [s1] "s"(s1), [s2] "s"(s2), [s3] "s"(s3) : "scc", "memory");
}
__device__ __forceinline__ bool allvalid(u32x4 v) { return __builtin_amdgcn_ballot_w64(max(max(v.x, v.y), max(v.z, v.w)) == SENT) == 0; }

struct Stream {
  rsrc_t sk, sv;
  u32 vk[4], vv[2];
  u32 ring;
  u32 ka[4], va[8];
  u32 goff;
  bf16x8 qf[4];
  f32x4 acc[8];
  float m, l;
};
__device__ __forceinline__ void issueK(const Stream& s, u32 so, u32 t) { dma4nt(s.ring + so, s.vk[0], s.vk[1], s.vk[2], s.vk[3], s.sk, s.goff + t * 16384u); }
__device__ __forceinline__ void issueV(const Stream& s, u32 so, u32 t) { dma4nt(s.ring + so + 4096, s.vv[0], s.vv[1], s.vv[0], s.vv[1], s.sv, s.goff + t * 16384u); }
__device__ __forceinline__ f32x4 qk(const Stream& s, u32 so) {
  bf16x8 kf[4];
#pragma unroll
  for (int i = 0; i < 4; ++i) kf[i] = *(LAS bf16x8*)(s.ka[i] + so);
  asm volatile("" : "+v"(kf[0]), "+v"(kf[1]), "+v"(kf[2]), "+v"(kf[3]));
  const f32x4 z = {0.f, 0.f, 0.f, 0.f};
  const f32x4 a = mma(kf[1], s.qf[1], mma(kf[0], s.qf[0], z)), b = mma(kf[3], s.qf[3], mma(kf[2], s.qf[2], z));
  return a + b;
}
__device__ __forceinline__ s16x4 softmax_step(Stream& s, f32x4 S) {
  float tm = fmaxf(fmaxf(S[0], S[1]), fmaxf(S[2], S[3]));
  tm = x16max(tm) * QSCALE;
  if (__builtin_amdgcn_ballot_w64(tm > s.m) != 0) {
    const float mn = fmaxf(s.m, tm);
    const float al = __builtin_amdgcn_exp2f(s.m - mn);
#pragma unroll
    for (int d = 0; d < 8; ++d) s.acc[d] *= al;
    s.l *= al; s.m = mn;
  }
  const float p0 = __builtin_amdgcn_exp2f(fmaf(S[0], QSCALE, -s.m)), p1 = __builtin_amdgcn_exp2f(fmaf(S[1], QSCALE, -s.m));
  const float p2 = __builtin_amdgcn_exp2f(fmaf(S[2], QSCALE, -s.m)), p3 = __builtin_amdgcn_exp2f(fmaf(S[3], QSCALE, -s.m));
  s.l += (p0 + p1) + (p2 + p3);
  const u32x2 pk = {pk_bf16(p0, p1), pk_bf16(p2, p3)};
  return __builtin_bit_cast(s16x4, pk);
}
__device__ __forceinline__ void readV(const Stream& s, u32 so, s16x4 (&vf)[8]) {
#pragma unroll
  for (int d = 0; d < 8; ++d) vf[d] = __builtin_amdgcn_ds_read_tr16_b64_v4i16((LAS s16x4*)(s.va[d] + so + 4096));
}
__device__ __forceinline__ void pv(Stream& s, const s16x4 (&vf)[8], s16x4 pb) {
#pragma unroll
  for (int d = 0; d < 8; ++d) s.acc[d] = __builtin_amdgcn_mfma_f32_16x16x16bf16_1k(vf[d], pb, s.acc[d], 0, 0, 0);
}

__global__ void __launch_bounds__(NTHR, 2) fused_decode_kernel(Params p) {
  extern __shared__ __attribute__((aligned(16))) char lds_raw[];
  const u32 lds = (u32)(size_t)(LAS char*)lds_raw;
  const int tid = threadIdx.x, lane = tid & 63;
  const u32 wave = rfl(tid >> 6);
  const u32 b = blockIdx.x, h = b & 7, j = b >> 3;
  const int g = lane >> 4, c = lane & 15, q4 = c >> 2, p4 = lane & 3;
  STAMP_DECL;
  STAMP(0);

  if (wave < 4) {
    Stream s;
    const size_t hb = (size_t)h * SK * 256;
    s.sk = mk_rsrc((const char*)p.k + hb, SK * 256);
    s.sv = mk_rsrc((const char*)p.v + hb, SK * 256);
#pragma unroll
    for (int i = 0; i < 4; ++i) s.vk[i] = g * 256 + 16 * (c ^ (4 * i + g));
#pragma unroll
    for (int i = 0; i < 2; ++i) s.vv[i] = g * 256 + 16 * (c ^ (((4 * i + g) & 7) << 1));
    s.ring = rfl(lds + L_RING + wave * RINGB);
    s.goff = rfl((j * 2048 + wave * 16) * 256);
    bar();
    __builtin_amdgcn_s_sleep(SDELAY - 1);
    issueK(s, 0, 0); issueV(s, 0, 0);
#pragma unroll
    for (int i = 0; i < 4; ++i) s.ka[i] = s.ring + 256 * c + 16 * ((4 * i + g) ^ c);
    {
      const int row = 4 * g + q4;
#pragma unroll
      for (int d = 0; d < 8; ++d) s.va[d] = s.ring + 256 * row + 32 * (d ^ (row & 7)) + 8 * p4;
    }
    for (u32 t = 1; t < NS; ++t) {
      WAITVM(4); issueK(s, t * 8192, t);
      WAITVM(4); issueV(s, t * 8192, t);
    }
    STAMP(1);
    bar();
    STAMP(2);
#pragma unroll
    for (int i = 0; i < 4; ++i) s.qf[i] = *(LAS bf16x8*)(lds + L_Q + ((c & 3) * 128 + 32 * i + 8 * g) * 2);
#pragma unroll
    for (int d = 0; d < 8; ++d) s.acc[d] = (f32x4){0.f, 0.f, 0.f, 0.f};
    s.m = -INFINITY; s.l = 0.f;
    WAITVM(0);
    u32 so = 0;
#pragma clang loop unroll(disable)
    for (u32 t = 0; t < NTILE; ++t) {
      asm volatile("" : "+s"(t));
      if (wave == 0 && lane == 0) *(volatile LAS u32*)(lds + L_PROG) = t;
      s16x4 vf[8];
      const u32 sn = so + 8192 == NS * 8192 ? 0 : so + 8192;
      if (t >= NS) { WAITVM(4); if (t + 1 < NTILE) issueK(s, sn, t + 1); }
      COMPUTE_QK;
      if (t == 0) issueK(s, 0, NS);
      if (t >= NS) { if (t + 1 < NTILE) { WAITVM(4); issueV(s, sn, t + 1); } else WAITVM(0); }
      COMPUTE_SM;
      if (t == 0) issueV(s, 0, NS);
      COMPUTE_PV;
      so = sn;
    }
    STAMP(3);
    const float lt = x16sum(s.l);
    if (c < 4) {
      LAS float* rec = (LAS float*)(s.ring) + c * 132;
#pragma unroll
      for (int d = 0; d < 8; ++d) *(LAS f32x4*)(rec + 16 * d + 4 * g) = s.acc[d];
      if (g == 0) { rec[128] = s.m; rec[129] = lt; }
    }
    bar();
    STAMP_FLUSH;
    return;
  }

  const u32 w = wave - 4;
  const rsrc_t rws = mk_rsrc(p.ws, W_END);
  const bool r_wo = j < 8, r_gu = j >= 8 && j < 15, r_dn = j >= 16 && j < 20;
  const u32 gi = h * 7 + (j - 8), cbk = h * 4 + (j - 16);
  const int row0 = 4 * g + q4;
  const u32 stg = rfl(lds + L_STG + w * 4096);
  u32 a128[4];
#pragma unroll
  for (int cb = 0; cb < 4; ++cb) a128[cb] = stg + row0 * 128 + 32 * (cb ^ ((row0 >> 1) & 3)) + 8 * p4;
  __builtin_amdgcn_s_setprio(2);
  {
    const rsrc_t rx = mk_rsrc(p.x, DM * 2), rq = mk_rsrc(p.wq, DM * 4096 * 2);
    dma1(rfl(lds + L_X + w * 1024), lane * 16, rx, 0);
    const u32 vq = (lane >> 3) * 8192 + src128(lane);
#pragma unroll
    for (int i = 0; i < 4; ++i) dma1(rfl(stg + i * 1024), vq, rq, rfl(((j >> 3) * 128 + w * 32 + i * 8) * 8192 + (h * 512 + 64 * (j & 7)) * 2));
  }
  const u64 E = __hip_atomic_load((GAS u64*)((GAS char*)p.ws + W_EPOCH), __ATOMIC_RELAXED, __HIP_MEMORY_SCOPE_AGENT);
  const u64 tag = (E + 1) & 0xFFFFFFFFFFull;
  const u32 taghi = (u32)(tag >> 8), taglo = (u32)(tag & 0xff);
  if (w == 0 && lane == 0) *(LAS u32x2*)(lds + L_CNT) = (u32x2){0u, 0u};
  bar();
  WAITVM(0);
  STAMP(1);
  {
    const u32x4 xv = *(LAS u32x4*)(lds + L_X + w * 1024 + lane * 16);
    float e[8] = {bf_lo(xv.x), bf_hi(xv.x), bf_lo(xv.y), bf_hi(xv.y), bf_lo(xv.z), bf_hi(xv.z), bf_lo(xv.w), bf_hi(xv.w)};
    float sm = 0.f;
#pragma unroll
    for (int i = 0; i < 8; ++i) sm += e[i];
    const float mean = wavesum(sm) * (1.f / DM);
    float sq = 0.f;
#pragma unroll
    for (int i = 0; i < 8; ++i) { e[i] -= mean; sq = fmaf(e[i], e[i], sq); }
    const float rs = __builtin_amdgcn_rsqf(wavesum(sq) * (1.f / DM) + 1e-5f);
    const u32 r0 = (j >> 3) * 128 + w * 32;
    bf16x8 wf[4];
#pragma unroll
    for (int cb = 0; cb < 4; ++cb) wf[cb] = wfrag<128>(a128[cb]);
    if ((u32)(lane >> 2) == (r0 >> 5)) {
      const u32x4 o = {pk_bf16(e[0] * rs, e[1] * rs), pk_bf16(e[2] * rs, e[3] * rs), pk_bf16(e[4] * rs, e[5] * rs), pk_bf16(e[6] * rs, e[7] * rs)};
      *(LAS u32x4*)(lds + L_XN + w * 64 + (lane & 3) * 16) = o;
    }
    waitlds();
    const bf16x8 xb = vfrag(lds + L_XN + w * 64 + g * 8);
    const f32x4 z = {0.f, 0.f, 0.f, 0.f};
#pragma unroll
    for (int cb = 0; cb < 4; ++cb) { const f32x4 r = mma(wf[cb], xb, z); if (c == 0) *(LAS f32x4*)(lds + L_P + w * 256 + cb * 64 + g * 16) = r; }
    waitlds();
    if (lane == 0) __hip_atomic_fetch_add((LAS u32*)(lds + L_CNT), 1u, __ATOMIC_RELAXED, __HIP_MEMORY_SCOPE_WORKGROUP);
  }
  if (w == 0) {
    for (u32 spin = 0; spin < SPIN_CAP; ++spin) { if (*(volatile LAS u32*)(lds + L_CNT) >= 4u) break; }
    asm volatile("" ::: "memory");
    if (lane < 32) {
      const LAS float* P = (const LAS float*)(lds + L_P) + 2 * lane;
      const float q0 = (P[0] + P[64]) + (P[128] + P[192]), q1 = (P[1] + P[65]) + (P[129] + P[193]);
      const u32x4 gr = {((__float_as_uint(q0) + 0x80u) >> 8) | (taglo << 24), taghi, ((__float_as_uint(q1) + 0x80u) >> 8) | (taglo << 24), taghi};
      st128_sc1(gr, rws, W_QG + ((h * 4 + (j >> 3)) * 512 + 64 * (j & 7) + 2 * lane) * 8);
    }
  }
  STAMP(2);
  {
    u32x4 qv[4];
#pragma unroll
    for (int i = 0; i < 4; ++i) qv[i] = (u32x4){0u, 0u, 0u, 0u};
    u32 pend = 0xfu;
    if (QSLP) __builtin_amdgcn_s_sleep(QSLP);
    for (u32 spin = 0; pend != 0 && spin < SPIN_CAP; ++spin) {
      poll4k(qv, pend, W_QG + h * 16384 + w * 1024 + lane * 16, rws, 4096, 8192, 12288);
#pragma unroll
      for (int i = 0; i < 4; ++i)
        if ((pend >> i) & 1) {
          const bool ok = qv[i].y == taghi && qv[i].w == taghi && (qv[i].x >> 24) == taglo && (qv[i].z >> 24) == taglo;
          if (__builtin_amdgcn_ballot_w64(!ok) == 0) pend &= ~(1u << i);
        }
    }
    const float a0 = (__uint_as_float(qv[0].x << 8) + __uint_as_float(qv[1].x << 8)) + (__uint_as_float(qv[2].x << 8) + __uint_as_float(qv[3].x << 8));
    const float a1 = (__uint_as_float(qv[0].z << 8) + __uint_as_float(qv[1].z << 8)) + (__uint_as_float(qv[2].z << 8) + __uint_as_float(qv[3].z << 8));
    *(LAS u32*)(lds + L_Q + (128 * w + 2 * lane) * 2) = pk_bf16(a0, a1);
  }
  STAMP(3);
  bar();
  __builtin_amdgcn_s_setprio(0);
  if (w == 0) {
    const u32x4 sent = {SENT, SENT, SENT, SENT};
#pragma unroll
    for (int i = 0; i < 2; ++i) { const u32 n = lane + 64 * i; st128_sc1(sent, rws, W_ACC + ((h * 4 + (n >> 5)) * 32 + j) * 512 + (n & 31) * 16); }
    if (lane < 4) st128_sc1(sent, rws, W_ML + ((h * 4 + lane) * 32 + j) * 16);
    else if (lane >= 16 && lane < 32) { if (r_wo) st128_sc1(sent, rws, W_YB + (h * 512 + 64 * j) * 4 + (lane - 16) * 16); }
    else if (lane >= 32 && lane < 36) { if (r_gu) st128_sc1(sent, rws, W_GU + gi * 64 + (lane - 32) * 16); }
    WAITVM(0);
    if (lane == 0) __hip_atomic_store((GAS u64*)((GAS char*)p.ws + W_FLAG) + b, tag, __ATOMIC_RELAXED, __HIP_MEMORY_SCOPE_AGENT);
  }
  bf16x8 wf[16];
  if (r_wo) {
    const rsrc_t ro = mk_rsrc(p.wo, 4096 * DM * 2);
    const u32 vo = (lane >> 3) * 1024 + src128(lane);
#pragma unroll
    for (int r = 0; r < 4; ++r) {
      waitlds();
#pragma unroll
      for (int i = 0; i < 4; ++i) dma1(rfl(stg + i * 1024), vo, ro, rfl((h * 512 + w * 128 + r * 32 + i * 8) * 1024 + 128 * j));
      WAITVM(0);
#pragma unroll
      for (int cb = 0; cb < 4; ++cb) wf[4 * r + cb] = wfrag<128>(a128[cb]);
    }
    waitlds();
  } else {
#pragma unroll
    for (int i = 0; i < 16; ++i) wf[i] = __builtin_bit_cast(bf16x8, (u32x4){0u, 0u, 0u, 0u});
  }
  if (w == 0 && (r_wo || r_gu || r_dn)) {
    for (u32 spin = 0; spin < SPIN_CAP; ++spin) { if (*(volatile LAS u32*)(lds + L_PROG) >= FLAGT) break; __builtin_amdgcn_s_sleep(32); }
    for (u32 spin = 0; spin < SPIN_CAP; ++spin) {
      bool ok = true;
      u64 f[4];
#pragma unroll
      for (int i = 0; i < 4; ++i) f[i] = __hip_atomic_load((GAS u64*)((GAS char*)p.ws + W_FLAG) + lane + 64 * i, __ATOMIC_RELAXED, __HIP_MEMORY_SCOPE_AGENT);
#pragma unroll
      for (int i = 0; i < 4; ++i) ok = ok && (f[i] == tag);
      if (__all(ok)) break;
      __builtin_amdgcn_s_sleep(64);
    }
    if (b == 0 && lane == 0) __hip_atomic_store((GAS u64*)((GAS char*)p.ws + W_EPOCH), E + 1, __ATOMIC_RELAXED, __HIP_MEMORY_SCOPE_AGENT);
  }
  STAMP(4);
  bar();
  STAMP(5);
  const u32 hh = w;
  {
    const LAS float* rec = (const LAS float*)(lds + L_RING) + hh * 132;
    float mw[4], lw[4];
#pragma unroll
    for (int k = 0; k < 4; ++k) { mw[k] = rec[k * (RINGB / 4) + 128]; lw[k] = rec[k * (RINGB / 4) + 129]; }
    const float M = fmaxf(fmaxf(mw[0], mw[1]), fmaxf(mw[2], mw[3]));
    f32x4 a = {0.f, 0.f, 0.f, 0.f}; float L = 0.f;
#pragma unroll
    for (int k = 0; k < 4; ++k) {
      const float sc = __builtin_amdgcn_exp2f(mw[k] - M);
      a += *(const LAS f32x4*)(rec + k * (RINGB / 4) + 4 * (lane & 31)) * sc; L = fmaf(sc, lw[k], L);
    }
    const u32x4 va = {fbits(a[0]), fbits(a[1]), fbits(a[2]), fbits(a[3])};
    const u32x4 vm = {fbits(M), fbits(L), 0u, 0u};
    if (lane < 33) st128_sc1(lane < 32 ? va : vm, rws, lane < 32 ? W_ACC + ((h * 4 + hh) * 32 + j) * 512 + lane * 16 : W_ML + ((h * 4 + hh) * 32 + j) * 16);
  }
  STAMP(6);
  const int half = lane >> 5, l5 = lane & 31;
  const f32x4 z4 = {0.f, 0.f, 0.f, 0.f};
  if (r_wo) {
    u32x4 av[16], ml = {0u, 0u, 0u, 0u};
#pragma unroll
    for (int i = 0; i < 16; ++i) av[i] = (u32x4){0u, 0u, 0u, 0u};
    u32 pend = 0x1ffffu;
    for (u32 spin = 0; pend != 0 && spin < SPIN_CAP; ++spin) {
      poll17(av, ml, pend, W_ACC + ((h * 4 + hh) * 32 + 16 * half) * 512 + l5 * 16, W_ML + ((h * 4 + hh) * 32 + l5) * 16, rws, 4096);
#pragma unroll
      for (int i = 0; i < 16; ++i) if ((pend >> i) & 1) { if (allvalid(av[i])) pend &= ~(1u << i); }
      if ((pend >> 16) & 1) { if (allvalid(ml)) pend &= ~(1u << 16); }
    }
    STAMP(7);
    const float ms = __uint_as_float(ml.x), ls = __uint_as_float(ml.y);
    const float M = x16max(row16max(ms));
    const float wt = __builtin_amdgcn_exp2f(ms - M);
    const float L = 0.5f * wavesum(wt * ls);
    LAS float* wg = (LAS float*)(lds + L_WGT) + hh * 32;
    if (lane < 32) wg[lane] = wt * __builtin_amdgcn_rcpf(L);
    waitlds();
    f32x4 o = {0.f, 0.f, 0.f, 0.f};
#pragma unroll
    for (int i4 = 0; i4 < 4; ++i4) {
      const f32x4 w4 = *(const LAS f32x4*)(wg + 16 * half + 4 * i4);
#pragma unroll
      for (int i = 0; i < 4; ++i) o += __builtin_bit_cast(f32x4, av[4 * i4 + i]) * w4[i];
    }
#pragma unroll
    for (int i = 0; i < 4; ++i) o[i] = x32sum(o[i]);
    if (lane < 32) *(LAS u32x2*)(lds + L_O + (hh * 128 + 4 * lane) * 2) = (u32x2){pk_bf16(o[0], o[1]), pk_bf16(o[2], o[3])};
    waitlds();
    f32x4 r[4] = {z4, z4, z4, z4};
#pragma unroll
    for (int k = 0; k < 4; ++k) {
      const bf16x8 vb = vfrag(lds + L_O + (hh * 128 + 32 * k + 4 * g) * 2);
#pragma unroll
      for (int cb = 0; cb < 4; ++cb) r[cb] = mma(wf[4 * k + cb], vb, r[cb]);
    }
    if (c == 0) {
#pragma unroll
      for (int cb = 0; cb < 4; ++cb) *(LAS f32x4*)(lds + L_P + w * 256 + cb * 64 + g * 16) = r[cb];
    }
    bar();
    if (w == 0 && lane < 16) {
      const LAS f32x4* P = (const LAS f32x4*)(lds + L_P);
      const f32x4 y = (P[lane] + P[16 + lane]) + (P[32 + lane] + P[48 + lane]);
      st128_sc1((u32x4){fbits(y[0]), fbits(y[1]), fbits(y[2]), fbits(y[3])}, rws, W_YB + (h * 512 + 64 * j) * 4 + lane * 16);
    }
    STAMP(8);
  } else if (r_gu) {
    const u32 wb = rfl(lds + L_RING + w * RINGB + 4096);
    {
      const rsrc_t rg = mk_rsrc(p.wg, DM * FF * 2), ru = mk_rsrc(p.wu, DM * FF * 2);
      const u32 vo = (lane >> 2) * (FF * 2) + src64(lane);
#pragma unroll
      for (int i = 0; i < 8; ++i) dma1(rfl(wb + i * 1024), vo, rg, rfl((w * 128 + i * 16) * (FF * 2) + gi * 64));
#pragma unroll
      for (int i = 0; i < 8; ++i) dma1(rfl(wb + 8192 + i * 1024), vo, ru, rfl((w * 128 + i * 16) * (FF * 2) + gi * 64));
      WAITVM(0);
#pragma unroll
      for (int k = 0; k < 4; ++k)
#pragma unroll
        for (int cb = 0; cb < 4; ++cb) wf[4 * k + cb] = wfrag<64>(wb + (cb >> 1) * 8192 + (32 * k + row0) * 64 + 32 * ((cb & 1) ^ ((row0 >> 2) & 1)) + 8 * p4);
    }
    STAMP(14);
    for (u32 spin = 0; spin < SPIN_CAP; ++spin) {
      const u32x4 t = ld128_sc1(rws, W_ML + ((h * 4 + w) * 32 + l5) * 16);
      if (allvalid(t)) break;
      __builtin_amdgcn_s_sleep(4);
      asm volatile("" ::: "memory");
    }
    u32x4 yv[4];
#pragma unroll
    for (int i = 0; i < 4; ++i) yv[i] = (u32x4){0u, 0u, 0u, 0u};
    u32 pend = 0xfu;
    for (u32 spin = 0; pend != 0 && spin < SPIN_CAP; ++spin) {
      poll4(yv, pend, W_YB + (4 * half * 512 + w * 128 + 4 * l5) * 4, rws, 4096);
#pragma unroll
      for (int i = 0; i < 4; ++i) if ((pend >> i) & 1) { if (allvalid(yv[i])) pend &= ~(1u << i); }
    }
    STAMP(9);
    f32x4 y = (__builtin_bit_cast(f32x4, yv[0]) + __builtin_bit_cast(f32x4, yv[1])) + (__builtin_bit_cast(f32x4, yv[2]) + __builtin_bit_cast(f32x4, yv[3]));
    const u32x2 xw = *(LAS u32x2*)(lds + L_X + w * 1024 + (w * 128 + 4 * l5) * 2);
    const float xe[4] = {bf_lo(xw.x), bf_hi(xw.x), bf_lo(xw.y), bf_hi(xw.y)};
    float hv[4];
#pragma unroll
    for (int i = 0; i < 4; ++i) hv[i] = rbf(xe[i] + rbf(x32sum(y[i])));
    const float mu = wavesum((hv[0] + hv[1]) + (hv[2] + hv[3])) * (1.f / 256.f);
    float m2 = 0.f;
#pragma unroll
    for (int i = 0; i < 4; ++i) m2 = fmaf(hv[i] - mu, hv[i] - mu, m2);
    m2 = 0.5f * wavesum(m2);
    if (lane == 0) { LAS float* st = (LAS float*)(lds + L_ST) + w * 2; st[0] = mu; st[1] = m2; }
    bar();
    const f32x4 s0 = *(LAS f32x4*)(lds + L_ST), s1 = *(LAS f32x4*)(lds + L_ST + 16);
    const float mean = ((s0[0] + s0[2]) + (s1[0] + s1[2])) * 0.25f;
    const float d0 = s0[0] - mean, d1 = s0[2] - mean, d2 = s1[0] - mean, d3 = s1[2] - mean;
    const float var = (((s0[1] + s0[3]) + (s1[1] + s1[3])) + 128.f * ((d0 * d0 + d1 * d1) + (d2 * d2 + d3 * d3))) * (1.f / DM);
    const float rs = __builtin_amdgcn_rsqf(var + 1e-5f);
    if (lane < 32) *(LAS u32x2*)(lds + L_XN + (w * 128 + 4 * lane) * 2) = (u32x2){pk_bf16((hv[0] - mean) * rs, (hv[1] - mean) * rs), pk_bf16((hv[2] - mean) * rs, (hv[3] - mean) * rs)};
    waitlds();
    f32x4 r[4] = {z4, z4, z4, z4};
#pragma unroll
    for (int k = 0; k < 4; ++k) {
      const bf16x8 vb = vfrag(lds + L_XN + (w * 128 + 32 * k + 4 * g) * 2);
#pragma unroll
      for (int cb = 0; cb < 4; ++cb) r[cb] = mma(wf[4 * k + cb], vb, r[cb]);
    }
    if (c == 0) {
#pragma unroll
      for (int cb = 0; cb < 4; ++cb) *(LAS f32x4*)(lds + L_P + w * 256 + cb * 64 + g * 16) = r[cb];
    }
    bar();
    if (w == 0 && lane < 16) {
      const LAS float* P = (const LAS float*)(lds + L_P);
      float gu[2];
#pragma unroll
      for (int e = 0; e < 2; ++e) {
        const int n = 2 * lane + e;
        const float gg = (P[n] + P[64 + n]) + (P[128 + n] + P[192 + n]);
        const float uu = (P[32 + n] + P[96 + n]) + (P[160 + n] + P[224 + n]);
        gu[e] = rbf(gg) * rbf(uu);
      }
      st32_sc1(nosent(pk_bf16(gu[0], gu[1])), rws, W_GU + gi * 64 + lane * 4);
    }
    STAMP(10);
  } else if (r_dn) {
    const u32 wb = rfl(lds + L_RING + w * RINGB + 4096);
    {
      const rsrc_t rd = mk_rsrc(p.wd, FF * DM * 2);
      const u32 vo = (lane >> 1) * 1024 + (lane & 1) * 16;
#pragma unroll
      for (int i = 0; i < 14; ++i) dma1(rfl(wb + i * 1024), vo, rd, rfl((w * 448 + i * 32) * 1024 + cbk * 32));
      WAITVM(0);
#pragma unroll
      for (int i = 0; i < 14; ++i) wf[i] = wfrag<32>(wb + (32 * i + row0) * 32 + 8 * p4);
    }
    STAMP(15);
    if (w == 0) {
      u32x4 yv;
      for (u32 spin = 0; spin < SPIN_CAP; ++spin) {
        yv = ld128_sc1(rws, W_YB + (((lane >> 2) & 7) * 512 + 16 * cbk + 4 * p4) * 4);
        if (allvalid(yv)) break;
        __builtin_amdgcn_s_sleep(2);
        asm volatile("" ::: "memory");
      }
      f32x4 y = __builtin_bit_cast(f32x4, yv);
#pragma unroll
      for (int i = 0; i < 4; ++i) {
        float t = y[i];
        t += dpp<ROR4>(t); t += dpp<XOR8>(t);
        auto sw = __builtin_amdgcn_permlane16_swap(__float_as_uint(t), __float_as_uint(t), false, false);
        y[i] = __uint_as_float(sw[0]) + __uint_as_float(sw[1]);
      }
      if (lane < 4) {
        const u32x2 xw = *(LAS u32x2*)(lds + L_X + (16 * cbk + 4 * lane) * 2);
        const f32x4 hv = {rbf(bf_lo(xw.x) + rbf(y[0])), rbf(bf_hi(xw.x) + rbf(y[1])), rbf(bf_lo(xw.y) + rbf(y[2])), rbf(bf_hi(xw.y) + rbf(y[3]))};
        *(LAS f32x4*)(lds + L_H + lane * 16) = hv;
      }
    } else {
      for (u32 spin = 0; spin < SPIN_CAP; ++spin) {
        const u32x4 t = ld128_sc1(rws, W_YB + (h * 512 + 16 * (cbk & 31) + 4 * p4) * 4);
        if (allvalid(t)) break;
        __builtin_amdgcn_s_sleep(4);
        asm volatile("" ::: "memory");
      }
    }
    u32x4 gv;
    const int gl = lane < 56 ? lane : 55;
    for (u32 spin = 0; spin < SPIN_CAP; ++spin) {
      gv = ld128_sc1(rws, W_GU + (w * 56 + gl) * 16);
      if (allvalid(gv)) break;
      asm volatile("" ::: "memory");
    }
    STAMP(12);
    if (lane < 56) *(LAS u32x4*)(lds + L_GU + (w * 56 + lane) * 16) = gv;
    waitlds();
    f32x4 r0 = z4, r1 = z4;
#pragma unroll
    for (int i = 0; i < 14; i += 2) {
      r0 = mma(wf[i], vfrag(lds + L_GU + (w * 448 + 32 * i + 4 * g) * 2), r0);
      r1 = mma(wf[i + 1], vfrag(lds + L_GU + (w * 448 + 32 * i + 32 + 4 * g) * 2), r1);
    }
    if (c == 0) *(LAS f32x4*)(lds + L_P + w * 256 + g * 16) = r0 + r1;
    bar();
    if (w == 0 && lane < 8) {
      const LAS float* P = (const LAS float*)(lds + L_P);
      const LAS float* H = (const LAS float*)(lds + L_H);
      float o[2];
#pragma unroll
      for (int e = 0; e < 2; ++e) { const int n = 2 * lane + e; o[e] = H[n] + rbf((P[n] + P[64 + n]) + (P[128 + n] + P[192 + n])); }
      *(GAS u32*)((GAS char*)p.out + (16 * cbk + 2 * lane) * 2) = pk_bf16(o[0], o[1]);
    }
    STAMP(13);
  }
  STAMP_FLUSH;
}

}

extern "C" void kernel_launch(void* const* d_in, const int* in_sizes, int n_in, void* d_out, int out_size, void* d_ws, size_t ws_size, hipStream_t stream) {
  (void)in_sizes; (void)n_in; (void)out_size; (void)ws_size;
  Params p;
  p.x = d_in[0]; p.k = d_in[1]; p.v = d_in[2]; p.wq = d_in[3]; p.wo = d_in[4];
  p.wg = d_in[6]; p.wu = d_in[7]; p.wd = d_in[8]; p.out = d_out; p.ws = d_ws;
  (void)hipFuncSetAttribute((const void*)fused_decode_kernel, hipFuncAttributeMaxDynamicSharedMemorySize, L_END);
  fused_decode_kernel<<<NWG, NTHR, L_END, stream>>>(p);
}
```
